```python
import torch

def _softmax(x, axis=-1):
    return torch.softmax(x, dim=axis)
def _gelu(x):
    return torch.nn.functional.gelu(x, approximate="tanh")
def _silu(x):
    return torch.nn.functional.silu(x)
def _sigmoid(x):
    return torch.sigmoid(x)
def _layernorm(x, axis=-1, eps=1e-5):
    m = x.mean(dim=axis, keepdim=True)
    v = x.var(dim=axis, keepdim=True, unbiased=False)
    return (x - m) / torch.sqrt(v + eps)
def _scatter_add(tgt, idx, src, axis=0):
    return tgt.clone().index_add_(axis, idx, src)
def _segment_sum(src, seg, n):
    out = torch.zeros((n,) + src.shape[1:], dtype=src.dtype)
    return out.index_add_(0, seg, src)
def _pad(x, pw):
    flat = tuple(v for pair in reversed(pw) for v in pair)
    return torch.nn.functional.pad(x, flat)

kernel_name = 'interleave_layers__sliding_local_dilated__glu_gated_swiglu__prompt'

def setup_inputs():

    _w = (lambda a: (lambda t: t.to(torch.bfloat16) if t.is_floating_point() else t)(torch.as_tensor(a)))
    def _A_setup():
        B, S, D = 1, 2048, 1024
        return {
            "x": _w((lambda *s: torch.randn(*s, dtype=torch.bfloat16) * 0.05)(B, S, D)),
            "wqkv": _w((lambda *s: torch.randn(*s, dtype=torch.bfloat16) * 0.05)(D, 3 * D)),
            "wo": _w((lambda *s: torch.randn(*s, dtype=torch.bfloat16) * 0.05)(D, D)),
        }
    _A_in = _A_setup()

    def _B_setup():
        B, S, D, F = 1, 2048, 1024, 3584
        return {
            "x": _w((lambda *s: torch.randn(*s, dtype=torch.bfloat16) * 0.05)(B, S, D)),
            "w1": _w((lambda *s: torch.randn(*s, dtype=torch.bfloat16) * 0.8)(D, F) * (2.0 / D) ** 0.5),
            "w3": _w((lambda *s: torch.randn(*s, dtype=torch.bfloat16) * 0.8)(D, F) * (2.0 / D) ** 0.5),
            "w2": _w((lambda *s: torch.randn(*s, dtype=torch.bfloat16) * 0.8)(F, D) * (2.0 / F) ** 0.5),
        }
    _B_in = _B_setup()

    return {
        "A_x": _A_in["x"],
        "A_wqkv": _A_in["wqkv"],
        "A_wo": _A_in["wo"],
        "B_x": _B_in["x"],
        "B_w1": _B_in["w1"],
        "B_w3": _B_in["w3"],
        "B_w2": _B_in["w2"],
    }

def reference(A_x, A_wqkv, A_wo, B_x, B_w1, B_w3, B_w2):
    def _A_ref(x, wqkv, wo):
        B, S, D = x.shape
        H = 32; Dh = D // H
        W, dil = 8, 2
        qkv = (lambda x, *d: x.permute(*d))((x @ wqkv).reshape(B, S, 3, H, Dh), 2, 0, 3, 1, 4)
        q, k, v = qkv[0], qkv[1], qkv[2]
        scale = Dh ** -0.5
        pos = torch.arange(S)
        off = torch.arange(W) * dil
        idx = pos[:, None] - off[None, :]
        mask = idx >= 0
        idx_c = torch.where(mask, idx, 0).reshape(-1)
        kg = k[:, :, idx_c, :].reshape(B, H, S, W, Dh)
        vg = v[:, :, idx_c, :].reshape(B, H, S, W, Dh)
        logits = torch.einsum("bhsd,bhswd->bhsw", q, kg) * scale
        logits = torch.where(mask[None, None, :, :], logits, -1e9)
        attn = _softmax(logits, axis=-1)
        o = torch.einsum("bhsw,bhswd->bhsd", attn, vg)
        return (lambda x, *d: x.permute(*d))(o, 0, 2, 1, 3).reshape(B, S, D) @ wo

    def _B_ref(x, w1, w3, w2):
        gate = torch.einsum("bsd,df->bsf", x, w1)
        up = torch.einsum("bsd,df->bsf", x, w3)
        h = _silu(gate) * up
        out = torch.einsum("bsf,fd->bsd", h, w2)
        return out

    x = A_x
    x = x + _A_ref(_layernorm(x, axis=-1), A_wqkv, A_wo)
    x = x + _B_ref(_layernorm(x, axis=-1), B_w1, B_w3, B_w2)
    x = x + _A_ref(_layernorm(x, axis=-1), A_wqkv, A_wo)
    x = x + _B_ref(_layernorm(x, axis=-1), B_w1, B_w3, B_w2)
    return x
```

```cpp
#include <hip/hip_runtime.h>
#include <hip/hip_bf16.h>
#include <stdint.h>

typedef __attribute__((ext_vector_type(8))) __bf16 bf16x8;
typedef __attribute__((ext_vector_type(4))) __bf16 bf16x4;
typedef __attribute__((ext_vector_type(4))) float f32x4;
typedef __attribute__((ext_vector_type(2))) float f32x2;
#define LDS3 __attribute__((address_space(3)))
#define GLB1 __attribute__((address_space(1)))

namespace {

constexpr int SEQ = 2048, DM = 1024, FF = 3584;
constexpr int NGRP = 32;

__device__ __forceinline__ float bfr(float x) { return (float)(__bf16)x; }

__device__ __forceinline__ void glds16(const void* g, LDS3 char* l) {
  __builtin_amdgcn_global_load_lds((const GLB1 void*)g, (LDS3 void*)l, 16, 0, 0);
}
__device__ __forceinline__ bf16x8 lds_b128(const LDS3 char* p) { return *(const LDS3 bf16x8*)p; }
__device__ __forceinline__ bf16x8 lds_tr2(const LDS3 char* p, int second_off) {
  bf16x4 lo = __builtin_amdgcn_ds_read_tr16_b64_v4bf16((LDS3 bf16x4*)p);
  bf16x4 hi = __builtin_amdgcn_ds_read_tr16_b64_v4bf16((LDS3 bf16x4*)(p + second_off));
  return __builtin_shufflevector(lo, hi, 0, 1, 2, 3, 4, 5, 6, 7);
}
template <int N> __device__ __forceinline__ void wait_vm() {
  asm volatile("s_waitcnt vmcnt(%0)" ::"i"(N) : "memory");
}
__device__ __forceinline__ void wait_lgkm0() { asm volatile("s_waitcnt lgkmcnt(0)" ::: "memory"); }
__device__ __forceinline__ void bar() { __builtin_amdgcn_s_barrier(); }

template <int R, int G, class Frag, class LoadStage, class ReadFrags, class Mma>
__device__ __forceinline__ void mainloop(int nstages, Frag& X, Frag& Y, LoadStage load_stage,
                                         ReadFrags read_frags, Mma mma) {
  read_frags(0, 0, X);
  int slot = 0;
  for (int s = 0; s < nstages; ++s) {
    read_frags(slot, 1, Y);
    mma(X);
    const int nslot = (slot + 1 == R) ? 0 : slot + 1;
    if (s + 1 < nstages) {
      wait_lgkm0();
      if (s + R - 1 < nstages) wait_vm<(R - 2) * G>(); else wait_vm<0>();
      bar();
      if (s + R < nstages) load_stage(s + R, slot);
      read_frags(nslot, 0, X);
    }
    mma(Y);
    slot = nslot;
  }
}

__device__ __forceinline__ f32x2 row_stats(const f32x2* __restrict__ st, int row) {
  const f32x4* p = (const f32x4*)(st + (size_t)row * NGRP);
  float ms[NGRP], m2 = 0.f, mean = 0.f;
#pragma unroll
  for (int i = 0; i < NGRP / 2; ++i) {
    f32x4 v = p[i];
    ms[2 * i] = v[0]; ms[2 * i + 1] = v[2];
    m2 += v[1] + v[3];
    mean += v[0] + v[2];
  }
  mean *= (1.f / NGRP);
  float d2 = 0.f;
#pragma unroll
  for (int i = 0; i < NGRP; ++i) { float d = ms[i] - mean; d2 += d * d; }
  const float var = (m2 + 32.f * d2) * (1.f / DM);
  const float mb = bfr(mean), vb = bfr(var);
  const float sd = bfr(sqrtf(bfr(vb + 1e-5f)));
  f32x2 r; r[0] = mb; r[1] = 1.f / sd;
  return r;
}

__global__ __launch_bounds__(256) void k_prep(const __bf16* __restrict__ x, const __bf16* __restrict__ wqkv,
                                              const __bf16* __restrict__ w1, const __bf16* __restrict__ w3,
                                              float* __restrict__ csum, f32x2* __restrict__ stats) {
  __shared__ float red[32][65];
  const int b = blockIdx.x, tid = threadIdx.x;
  if (b < 160) {
    const __bf16* W; int ld, col0, out0;
    if (b < 48) { W = wqkv; ld = 3 * DM; col0 = 64 * b; out0 = col0; }
    else if (b < 104) { W = w1; ld = FF; col0 = 64 * (b - 48); out0 = 3 * DM + col0; }
    else { W = w3; ld = FF; col0 = 64 * (b - 104); out0 = 3 * DM + FF + col0; }
    const int cc = tid & 7, rp = tid >> 3;
    float acc[8] = {0, 0, 0, 0, 0, 0, 0, 0};
#pragma unroll 4
    for (int r = rp; r < DM; r += 32) {
      bf16x8 v = *(const bf16x8*)(W + (size_t)r * ld + col0 + 8 * cc);
#pragma unroll
      for (int j = 0; j < 8; ++j) acc[j] += (float)v[j];
    }
#pragma unroll
    for (int j = 0; j < 8; ++j) red[rp][8 * cc + j] = acc[j];
    __syncthreads();
    if (tid < 64) {
      float s = 0.f;
#pragma unroll
      for (int i = 0; i < 32; ++i) s += red[i][tid];
      csum[out0 + tid] = s;
    }
  } else {
    const int id = (b - 160) * 256 + tid, row = id >> 5, grp = id & 31;
    const bf16x8* p = (const bf16x8*)(x + (size_t)row * DM + 32 * grp);
    float v[32]; float s = 0.f;
#pragma unroll
    for (int i = 0; i < 4; ++i) {
      bf16x8 t = p[i];
#pragma unroll
      for (int j = 0; j < 8; ++j) { v[8 * i + j] = (float)t[j]; s += v[8 * i + j]; }
    }
    const float mean = s * (1.f / 32.f);
    float m2 = 0.f;
#pragma unroll
    for (int i = 0; i < 32; ++i) { float d = v[i] - mean; m2 += d * d; }
    f32x2 r; r[0] = mean; r[1] = m2;
    stats[(size_t)row * NGRP + grp] = r;
  }
}

namespace p1 {
constexpr int R = 3;
constexpr int A_BYTES = 144 * 128, B_BYTES = 3 * 8192, SLOT = A_BYTES + B_BYTES;
constexpr int QS = 400;
constexpr int STAT_OFF = R * SLOT;
constexpr int LDS_BYTES = STAT_OFF + 144 * 8;
struct Frag { bf16x8 a[4]; bf16x8 ah; bf16x8 b[3]; };
}

__global__ __launch_bounds__(512, 2) void k_qkv_attn(const __bf16* __restrict__ x, const f32x2* __restrict__ stats,
                                                     const float* __restrict__ csum, const __bf16* __restrict__ wqkv,
                                                     __bf16* __restrict__ o) {
  using namespace p1;
  extern __shared__ __attribute__((aligned(16))) char smem_[];
  LDS3 char* sm = (LDS3 char*)smem_;
  const int tid = threadIdx.x, lane = tid & 63;
  const int w = __builtin_amdgcn_readfirstlane(tid >> 6);
  const int wm = w >> 2, wn = w & 3;
  const int b = blockIdx.x, xcd = b & 7, idx = b >> 3;
  const int mt = 8 * (xcd & 1) + (idx & 7), nt = 4 * (xcd >> 1) + (idx >> 3);
  const int m0 = mt * 128;

  uint32_t aoff[3], boff[3];
#pragma unroll
  for (int n = 0; n < 3; ++n) {
    const int j = w + 8 * n, row = 8 * j + (lane >> 3), c = (lane & 7) ^ (lane >> 3);
    int grow = m0 - 16 + row; grow = grow < 0 ? 0 : grow;
    aoff[n] = (uint32_t)(grow * DM + 8 * c) * 2u;
  }
#pragma unroll
  for (int n = 0; n < 3; ++n) {
    const int ii = 3 * w + n, panel = ii >> 3, blk = ii & 7;
    const int k = 8 * blk + (lane >> 3), f = (k & 2) | ((k >> 1) & 4), c = (lane & 7) ^ f;
    boff[n] = (uint32_t)(k * (3 * DM) + panel * DM + 64 * nt + 8 * c) * 2u;
  }
  auto load_stage = [&](int s, int slot) {
    LDS3 char* sb = sm + slot * SLOT;
    const char* ga = (const char*)x + s * 128;
#pragma unroll
    for (int n = 0; n < 3; ++n)
      if (n < 2 || w < 2) glds16(ga + aoff[n], sb + 1024 * (w + 8 * n));
    const char* gb = (const char*)wqkv + (size_t)s * (64 * 3 * DM * 2);
#pragma unroll
    for (int n = 0; n < 3; ++n) glds16(gb + boff[n], sb + A_BYTES + 1024 * (3 * w + n));
  };
  const int g = lane >> 4, q = (lane & 15) >> 2, p = lane & 3;
  const int laneA0 = 128 * (lane & 15) + 16 * (g ^ (lane & 7));
  const int laneA1 = laneA0 ^ 64;
  const int fh = ((q >> 1) & 1) + 2 * (g & 1);
  const int laneB = 128 * (8 * g + q) + 32 * (wn ^ fh) + 16 * (p >> 1) + 8 * (p & 1);
  auto read_frags = [&](int slot, int kk, Frag& F) {
    const LDS3 char* sa = sm + slot * SLOT + (kk ? laneA1 : laneA0);
#pragma unroll
    for (int i = 0; i < 4; ++i) F.a[i] = lds_b128(sa + 2048 * (1 + 4 * wm + i));
    F.ah = lds_b128(sa);
    const LDS3 char* sb = sm + slot * SLOT + A_BYTES + laneB + 4096 * kk;
#pragma unroll
    for (int pn = 0; pn < 3; ++pn) F.b[pn] = lds_tr2(sb + 8192 * pn, 512);
  };
  f32x4 acc[3][4], acch;
#pragma unroll
  for (int pn = 0; pn < 3; ++pn)
#pragma unroll
    for (int i = 0; i < 4; ++i) acc[pn][i] = f32x4{0, 0, 0, 0};
  acch = f32x4{0, 0, 0, 0};
  auto mma = [&](Frag& F) {
#pragma unroll
    for (int pn = 0; pn < 3; ++pn)
#pragma unroll
      for (int i = 0; i < 4; ++i)
        acc[pn][i] = __builtin_amdgcn_mfma_f32_16x16x32_bf16(F.b[pn], F.a[i], acc[pn][i], 0, 0, 0);
    const bf16x8 bh = wm ? F.b[2] : F.b[1];
    acch = __builtin_amdgcn_mfma_f32_16x16x32_bf16(bh, F.ah, acch, 0, 0, 0);
  };

#pragma unroll
  for (int s = 0; s < R; ++s) load_stage(s, s);
  LDS3 f32x2* rowst = (LDS3 f32x2*)(sm + STAT_OFF);
  if (tid < 144) {
    int grow = m0 - 16 + tid; grow = grow < 0 ? 0 : grow;
    rowst[tid] = row_stats(stats, grow);
  }
  wait_vm<0>();
  __syncthreads();
  Frag X, Y;
  mainloop<R, 5>(DM / 64, X, Y, load_stage, read_frags, mma);
  wait_lgkm0();
  __syncthreads();

  {
    const int nl = 4 * g;
#pragma unroll
    for (int pn = 0; pn < 3; ++pn) {
      const f32x4 cs = *(const f32x4*)(csum + pn * DM + 64 * nt + 16 * wn + nl);
#pragma unroll
      for (int i = 0; i < 4; ++i) {
        const int row = 16 + 64 * wm + 16 * i + (lane & 15);
        const f32x2 rs = rowst[row];
        bf16x4 ov;
#pragma unroll
        for (int r = 0; r < 4; ++r) ov[r] = (__bf16)((acc[pn][i][r] - rs[0] * cs[r]) * rs[1]);
        *(LDS3 bf16x4*)(sm + row * QS + (64 * pn + 16 * wn + nl) * 2) = ov;
      }
    }
    {
      const int pn = wm ? 2 : 1;
      const f32x4 cs = *(const f32x4*)(csum + pn * DM + 64 * nt + 16 * wn + nl);
      const int row = lane & 15;
      const f32x2 rs = rowst[row];
      bf16x4 ov;
#pragma unroll
      for (int r = 0; r < 4; ++r) ov[r] = (__bf16)((acch[r] - rs[0] * cs[r]) * rs[1]);
      *(LDS3 bf16x4*)(sm + row * QS + (64 * pn + 16 * wn + nl) * 2) = ov;
    }
  }
  __syncthreads();

  {
    const int row = tid >> 2, head = (tid >> 1) & 1, half = tid & 1;
    const int s = m0 + row;
    const int cb = (32 * head + 16 * half) * 2;
    const LDS3 char* qp = sm + (16 + row) * QS + cb;
    const bf16x8 q0 = lds_b128(qp), q1 = lds_b128(qp + 16);
    float qf[16];
#pragma unroll
    for (int j = 0; j < 8; ++j) { qf[j] = (float)q0[j]; qf[8 + j] = (float)q1[j]; }
    float lg[8];
#pragma unroll
    for (int wi = 0; wi < 8; ++wi) {
      const LDS3 char* kp = sm + (16 + row - 2 * wi) * QS + 128 + cb;
      const bf16x8 k0 = lds_b128(kp), k1 = lds_b128(kp + 16);
      float d = 0.f;
#pragma unroll
      for (int j = 0; j < 8; ++j) { d += qf[j] * (float)k0[j]; d += qf[8 + j] * (float)k1[j]; }
      d += __shfl_xor(d, 1);
      const float l = bfr(bfr(d) * 0.17677669529663687f);
      lg[wi] = (s - 2 * wi >= 0) ? l : -1e9f;
    }
    float mx = lg[0];
#pragma unroll
    for (int wi = 1; wi < 8; ++wi) mx = fmaxf(mx, lg[wi]);
    float e[8], sum = 0.f;
#pragma unroll
    for (int wi = 0; wi < 8; ++wi) { e[wi] = __expf(lg[wi] - mx); sum += e[wi]; }
    const float inv = 1.f / sum;
    float ov[16];
#pragma unroll
    for (int j = 0; j < 16; ++j) ov[j] = 0.f;
#pragma unroll
    for (int wi = 0; wi < 8; ++wi) {
      const float pw = bfr(e[wi] * inv);
      const LDS3 char* vp = sm + (16 + row - 2 * wi) * QS + 256 + cb;
      const bf16x8 v0 = lds_b128(vp), v1 = lds_b128(vp + 16);
#pragma unroll
      for (int j = 0; j < 8; ++j) { ov[j] += pw * (float)v0[j]; ov[8 + j] += pw * (float)v1[j]; }
    }
    bf16x8 o0, o1;
#pragma unroll
    for (int j = 0; j < 8; ++j) { o0[j] = (__bf16)ov[j]; o1[j] = (__bf16)ov[8 + j]; }
    bf16x8* op = (bf16x8*)(o + (size_t)s * DM + 64 * nt + 32 * head + 16 * half);
    op[0] = o0; op[1] = o1;
  }
}

namespace p2 {
constexpr int R = 4;
constexpr int A_BYTES = 128 * 128, B_BYTES = 8192, SLOT = A_BYTES + B_BYTES;
constexpr int LDS_BYTES = R * SLOT;
struct Frag { bf16x8 a[4]; bf16x8 b[2]; };
}

__global__ __launch_bounds__(256, 1) void k_proj_res(const __bf16* __restrict__ A, const __bf16* __restrict__ W, int K,
                                                     const __bf16* __restrict__ xin, __bf16* __restrict__ xout,
                                                     f32x2* __restrict__ stats) {
  using namespace p2;
  extern __shared__ __attribute__((aligned(16))) char smem_[];
  LDS3 char* sm = (LDS3 char*)smem_;
  const int tid = threadIdx.x, lane = tid & 63;
  const int w = __builtin_amdgcn_readfirstlane(tid >> 6);
  const int wm = w >> 1, wn = w & 1;
  const int b = blockIdx.x, xcd = b & 7, idx = b >> 3;
  const int mt = 8 * (xcd & 1) + (idx & 7), nt = 4 * (xcd >> 1) + (idx >> 3);
  const int m0 = mt * 128, n0 = nt * 64;

  uint32_t aoff[4], boff[2];
#pragma unroll
  for (int n = 0; n < 4; ++n) {
    const int j = 4 * w + n, row = 8 * j + (lane >> 3), c = (lane & 7) ^ (lane >> 3);
    aoff[n] = (uint32_t)((m0 + row) * K + 8 * c) * 2u;
  }
#pragma unroll
  for (int n = 0; n < 2; ++n) {
    const int blk = 2 * w + n, k = 8 * blk + (lane >> 3), f = (k & 2) | ((k >> 1) & 4), c = (lane & 7) ^ f;
    boff[n] = (uint32_t)(k * DM + n0 + 8 * c) * 2u;
  }
  auto load_stage = [&](int s, int slot) {
    LDS3 char* sb = sm + slot * SLOT;
    const char* ga = (const char*)A + s * 128;
#pragma unroll
    for (int n = 0; n < 4; ++n) glds16(ga + aoff[n], sb + 1024 * (4 * w + n));
    const char* gb = (const char*)W + (size_t)s * (64 * DM * 2);
#pragma unroll
    for (int n = 0; n < 2; ++n) glds16(gb + boff[n], sb + A_BYTES + 1024 * (2 * w + n));
  };
  const int g = lane >> 4, q = (lane & 15) >> 2, p = lane & 3;
  const int laneA0 = 128 * (lane & 15) + 16 * (g ^ (lane & 7));
  const int laneA1 = laneA0 ^ 64;
  const int fh = ((q >> 1) & 1) + 2 * (g & 1);
  int laneB[2];
#pragma unroll
  for (int jn = 0; jn < 2; ++jn) laneB[jn] = 128 * (8 * g + q) + 32 * ((2 * wn + jn) ^ fh) + 16 * (p >> 1) + 8 * (p & 1);
  auto read_frags = [&](int slot, int kk, Frag& F) {
    const LDS3 char* sa = sm + slot * SLOT + (kk ? laneA1 : laneA0);
#pragma unroll
    for (int i = 0; i < 4; ++i) F.a[i] = lds_b128(sa + 2048 * (4 * wm + i));
    const LDS3 char* sb = sm + slot * SLOT + A_BYTES + 4096 * kk;
#pragma unroll
    for (int jn = 0; jn < 2; ++jn) F.b[jn] = lds_tr2(sb + laneB[jn], 512);
  };
  f32x4 acc[2][4];
#pragma unroll
  for (int jn = 0; jn < 2; ++jn)
#pragma unroll
    for (int i = 0; i < 4; ++i) acc[jn][i] = f32x4{0, 0, 0, 0};
  auto mma = [&](Frag& F) {
#pragma unroll
    for (int jn = 0; jn < 2; ++jn)
#pragma unroll
      for (int i = 0; i < 4; ++i)
        acc[jn][i] = __builtin_amdgcn_mfma_f32_16x16x32_bf16(F.b[jn], F.a[i], acc[jn][i], 0, 0, 0);
  };
  const int nstages = K / 64;
#pragma unroll
  for (int s = 0; s < R; ++s) load_stage(s, s);
  wait_vm<(R - 1) * 6>();
  bar();
  Frag X, Y;
  mainloop<R, 6>(nstages, X, Y, load_stage, read_frags, mma);

#pragma unroll
  for (int i = 0; i < 4; ++i) {
    const int row = m0 + 64 * wm + 16 * i + (lane & 15);
    float v[8];
#pragma unroll
    for (int jn = 0; jn < 2; ++jn) {
      const size_t off = (size_t)row * DM + n0 + 32 * wn + 16 * jn + 4 * g;
      const bf16x4 xo = *(const bf16x4*)(xin + off);
      bf16x4 xn;
#pragma unroll
      for (int r = 0; r < 4; ++r) {
        xn[r] = (__bf16)((float)xo[r] + bfr(acc[jn][i][r]));
        v[4 * jn + r] = (float)xn[r];
      }
      *(bf16x4*)(xout + off) = xn;
    }
    float s = 0.f;
#pragma unroll
    for (int j = 0; j < 8; ++j) s += v[j];
    s += __shfl_xor(s, 16); s += __shfl_xor(s, 32);
    const float mean = s * (1.f / 32.f);
    float m2 = 0.f;
#pragma unroll
    for (int j = 0; j < 8; ++j) { const float d = v[j] - mean; m2 += d * d; }
    m2 += __shfl_xor(m2, 16); m2 += __shfl_xor(m2, 32);
    if (g == 0) { f32x2 r; r[0] = mean; r[1] = m2; stats[(size_t)row * NGRP + 2 * nt + wn] = r; }
  }
}

namespace p3 {
constexpr int R = 2;
constexpr int A_BYTES = 128 * 128, G_BYTES = 64 * 448, U_OFF = G_BYTES + 16;
constexpr int SLOT = A_BYTES + U_OFF + G_BYTES;
constexpr int STAT_OFF = R * SLOT;
constexpr int LDS_BYTES = STAT_OFF + 128 * 8;
}

__global__ __launch_bounds__(512, 2) void k_ffn_up(const __bf16* __restrict__ x, const f32x2* __restrict__ stats,
                                                   const float* __restrict__ csum13, const __bf16* __restrict__ w1,
                                                   const __bf16* __restrict__ w3, __bf16* __restrict__ h) {
  using namespace p3;
  extern __shared__ __attribute__((aligned(16))) char smem_[];
  LDS3 char* sm = (LDS3 char*)smem_;
  const int tid = threadIdx.x, lane = tid & 63;
  const int w = __builtin_amdgcn_readfirstlane(tid >> 6);
  const int wm = w >> 2, wn = w & 3;
  const int b = blockIdx.x, xcd = b & 7, idx = b >> 3;
  const int mt = 8 * (xcd & 1) + (idx & 7), nt = 4 * (xcd >> 1) + (idx >> 3);
  const int m0 = mt * 128, n0 = nt * 224;

  uint32_t aoff[2], boff[7];
#pragma unroll
  for (int n = 0; n < 2; ++n) {
    const int j = 2 * w + n, row = 8 * j + (lane >> 3), c = (lane & 7) ^ (lane >> 3);
    aoff[n] = (uint32_t)((m0 + row) * DM + 8 * c) * 2u;
  }
  const __bf16* wsrc = (w >> 2) ? w3 : w1;
#pragma unroll
  for (int n = 0; n < 7; ++n) {
    const int id = 64 * (7 * (w & 3) + n) + lane, k = id / 28, cp = id % 28, c = cp ^ (2 * ((k >> 3) & 1));
    boff[n] = (uint32_t)(k * FF + n0 + 8 * c) * 2u;
  }
  auto load_stage = [&](int s, int slot) {
    LDS3 char* sb = sm + slot * SLOT;
    const char* ga = (const char*)x + s * 128;
#pragma unroll
    for (int n = 0; n < 2; ++n) glds16(ga + aoff[n], sb + 1024 * (2 * w + n));
    const char* gb = (const char*)wsrc + (size_t)s * (64 * FF * 2);
    LDS3 char* bb = sb + A_BYTES + (w >> 2) * U_OFF;
#pragma unroll
    for (int n = 0; n < 7; ++n) glds16(gb + boff[n], bb + 1024 * (7 * (w & 3) + n));
  };
  const int g = lane >> 4, q = (lane & 15) >> 2, p = lane & 3;
  const int laneA0 = 128 * (lane & 15) + 16 * (g ^ (lane & 7));
  const int laneA1 = laneA0 ^ 64;
  int laneB[7];
#pragma unroll
  for (int jj = 0; jj < 7; ++jj)
    laneB[jj] = (p < 2 ? 0 : U_OFF) + 448 * (8 * g + q) + 16 * ((7 * wn + jj) ^ (2 * (g & 1))) + 8 * (p & 1);
  auto rdA = [&](int slot, int kk, bf16x8 (&a)[4]) {
    const LDS3 char* sa = sm + slot * SLOT + (kk ? laneA1 : laneA0);
#pragma unroll
    for (int i = 0; i < 4; ++i) a[i] = lds_b128(sa + 2048 * (4 * wm + i));
  };
  auto rdB = [&](int slot, int kk, int jj) -> bf16x8 {
    return lds_tr2(sm + slot * SLOT + A_BYTES + 14336 * kk + laneB[jj], 1792);
  };
  f32x4 acc[7][4];
#pragma unroll
  for (int jj = 0; jj < 7; ++jj)
#pragma unroll
    for (int i = 0; i < 4; ++i) acc[jj][i] = f32x4{0, 0, 0, 0};
#pragma unroll
  for (int s = 0; s < R; ++s) load_stage(s, s);
  LDS3 f32x2* rowst = (LDS3 f32x2*)(sm + STAT_OFF);
  if (tid < 128) rowst[tid] = row_stats(stats, m0 + tid);
  wait_vm<0>();
  __syncthreads();
  {
    constexpr int NS = DM / 64;
    bf16x8 A0[4], A1[4];
    rdA(0, 0, A0);
    int slot = 0;
    for (int s = 0; s < NS; ++s) {
      const int nslot = slot ^ 1;
      rdA(slot, 1, A1);
      {
        bf16x8 b0 = rdB(slot, 0, 0), b1 = rdB(slot, 0, 1);
#pragma unroll
        for (int jj = 0; jj < 7; ++jj) {
          bf16x8 bn = b1;
          if (jj + 2 < 7) bn = rdB(slot, 0, jj + 2);
#pragma unroll
          for (int i = 0; i < 4; ++i)
            acc[jj][i] = __builtin_amdgcn_mfma_f32_16x16x32_bf16(b0, A0[i], acc[jj][i], 0, 0, 0);
          b0 = b1; b1 = bn;
          __builtin_amdgcn_sched_barrier(0);
        }
      }
      {
        bf16x8 b0 = rdB(slot, 1, 0), b1 = rdB(slot, 1, 1);
#pragma unroll
        for (int jj = 0; jj < 7; ++jj) {
          bf16x8 bn = b1;
          if (jj + 2 < 7) bn = rdB(slot, 1, jj + 2);
          if (jj == 5 && s + 1 < NS) {
            wait_lgkm0();
            wait_vm<0>();
            bar();
            if (s + 2 < NS) load_stage(s + 2, slot);
            rdA(nslot, 0, A0);
          }
#pragma unroll
          for (int i = 0; i < 4; ++i)
            acc[jj][i] = __builtin_amdgcn_mfma_f32_16x16x32_bf16(b0, A1[i], acc[jj][i], 0, 0, 0);
          b0 = b1; b1 = bn;
          __builtin_amdgcn_sched_barrier(0);
        }
      }
      slot = nslot;
    }
  }

  const int hi = lane >> 5, cq = 4 * (g & 1);
#pragma unroll
  for (int jj = 0; jj < 7; jj += 2) {
    const bool pair = (jj + 1 < 7);
    const int jb = pair ? jj + 1 : jj;
    const f32x4 csa = *(const f32x4*)(csum13 + hi * FF + n0 + 8 * (7 * wn + jj) + cq);
    const f32x4 csb = *(const f32x4*)(csum13 + hi * FF + n0 + 8 * (7 * wn + jb) + cq);
#pragma unroll
    for (int i = 0; i < 4; ++i) {
      const int lrow = 64 * wm + 16 * i + (lane & 15);
      const f32x2 rs = rowst[lrow];
      __bf16* hrow = h + (size_t)(m0 + lrow) * FF + n0 + 56 * wn + cq;
      bf16x4 hv;
#pragma unroll
      for (int r = 0; r < 4; ++r) {
        const float va = (acc[jj][i][r] - rs[0] * csa[r]) * rs[1];
        const float vb = (acc[jb][i][r] - rs[0] * csb[r]) * rs[1];
        unsigned ua = __builtin_bit_cast(unsigned, va), ub = __builtin_bit_cast(unsigned, vb);
        asm volatile("s_nop 1\n\tv_permlane32_swap_b32 %0, %1" : "+v"(ua), "+v"(ub));
        const float gt = bfr(__builtin_bit_cast(float, ua));
        const float up = bfr(__builtin_bit_cast(float, ub));
        const float sg = bfr(gt / (1.f + __expf(-gt)));
        hv[r] = (__bf16)(sg * up);
      }
      if (pair || hi == 0) *(bf16x4*)(hrow + 8 * (jj + hi)) = hv;
    }
  }
}

}

extern "C" void kernel_launch(void* const* d_in, const int* in_sizes, int n_in, void* d_out, int out_size, void* d_ws,
                              size_t ws_size, hipStream_t stream) {
  const __bf16* x0 = (const __bf16*)d_in[0];
  const __bf16* wqkv = (const __bf16*)d_in[1];
  const __bf16* wo = (const __bf16*)d_in[2];
  const __bf16* w1 = (const __bf16*)d_in[4];
  const __bf16* w3 = (const __bf16*)d_in[5];
  const __bf16* w2 = (const __bf16*)d_in[6];
  __bf16* out = (__bf16*)d_out;

  char* ws = (char*)d_ws;
  __bf16* o = (__bf16*)(ws);
  __bf16* xa = (__bf16*)(ws + (4 << 20));
  __bf16* xb = (__bf16*)(ws + (8 << 20));
  __bf16* h = (__bf16*)(ws + (12 << 20));
  f32x2* stats = (f32x2*)(ws + (28 << 20));
  float* csum = (float*)(ws + (29 << 20));

  hipFuncSetAttribute((const void*)k_qkv_attn, hipFuncAttributeMaxDynamicSharedMemorySize, p1::LDS_BYTES);
  hipFuncSetAttribute((const void*)k_proj_res, hipFuncAttributeMaxDynamicSharedMemorySize, p2::LDS_BYTES);
  hipFuncSetAttribute((const void*)k_ffn_up, hipFuncAttributeMaxDynamicSharedMemorySize, p3::LDS_BYTES);

  k_prep<<<416, 256, 0, stream>>>(x0, wqkv, w1, w3, csum, stats);
  const __bf16* xcur = x0;
  for (int layer = 0; layer < 2; ++layer) {
    __bf16* x1 = xa;
    __bf16* x2 = layer ? out : xb;
    k_qkv_attn<<<256, 512, p1::LDS_BYTES, stream>>>(xcur, stats, csum, wqkv, o);
    k_proj_res<<<256, 256, p2::LDS_BYTES, stream>>>(o, wo, DM, xcur, x1, stats);
    k_ffn_up<<<256, 512, p3::LDS_BYTES, stream>>>(x1, stats, csum + 3 * DM, w1, w3, h);
    k_proj_res<<<256, 256, p2::LDS_BYTES, stream>>>(h, w2, FF, x1, x2, stats);
    xcur = x2;
  }
}
```

```cpp
#include <hip/hip_runtime.h>
#include <hip/hip_bf16.h>
#include <stdint.h>

typedef __attribute__((ext_vector_type(8))) __bf16 bf16x8;
typedef __attribute__((ext_vector_type(4))) __bf16 bf16x4;
typedef __attribute__((ext_vector_type(4))) float f32x4;
typedef __attribute__((ext_vector_type(2))) float f32x2;
#define LDS3 __attribute__((address_space(3)))
#define GLB1 __attribute__((address_space(1)))

#define ROT 3
namespace {

constexpr int SEQ = 2048, DM = 1024, FF = 3584;
constexpr int NGRP = 32;

__device__ __forceinline__ float bfr(float x) { return (float)(__bf16)x; }

__device__ __forceinline__ void glds16(const void* base, uint32_t voff, LDS3 char* l) {
  unsigned keep;
  const unsigned la = __builtin_amdgcn_readfirstlane((unsigned)(size_t)l);
  asm volatile("s_mov_b32 %0, m0\n\ts_mov_b32 m0, %3\n\ts_nop 0\n\tglobal_load_lds_dwordx4 %1, %2\n\ts_mov_b32 m0, %0"
               : "=&s"(keep) : "v"(voff), "s"(base), "s"(la) : "memory");
}
__device__ __forceinline__ bf16x8 lds_b128(const LDS3 char* p) { return *(const LDS3 bf16x8*)p; }
__device__ __forceinline__ bf16x8 lds_tr2(const LDS3 char* p, int second_off) {
  bf16x4 lo = __builtin_amdgcn_ds_read_tr16_b64_v4bf16((LDS3 bf16x4*)p);
  bf16x4 hi = __builtin_amdgcn_ds_read_tr16_b64_v4bf16((LDS3 bf16x4*)(p + second_off));
  return __builtin_shufflevector(lo, hi, 0, 1, 2, 3, 4, 5, 6, 7);
}
template <int N> __device__ __forceinline__ void wait_vm() {
  asm volatile("s_waitcnt vmcnt(%0)" ::"i"(N) : "memory");
}
__device__ __forceinline__ void wait_lgkm0() { asm volatile("s_waitcnt lgkmcnt(0)" ::: "memory"); }
__device__ __forceinline__ void bar() { __builtin_amdgcn_s_barrier(); }
#define TS(kid, i) do {} while (0)

template <int R, int G, class Frag, class LoadStage, class ReadFrags, class Mma>
__device__ __forceinline__ void mainloop(int nstages, Frag& X, Frag& Y, LoadStage load_stage,
                                         ReadFrags read_frags, Mma mma) {
  read_frags(0, 0, X);
  int slot = 0;
  for (int s = 0; s < nstages; ++s) {
    read_frags(slot, 1, Y);
    mma(X);
    const int nslot = (slot + 1 == R) ? 0 : slot + 1;
    if (s + 1 < nstages) {
      wait_lgkm0();
      if (s + R - 1 < nstages) wait_vm<(R - 2) * G>(); else wait_vm<0>();
      bar();
      if (s + R < nstages) load_stage(s + R, slot);
      read_frags(nslot, 0, X);
    }
    mma(Y);
    slot = nslot;
  }
}

__device__ __forceinline__ f32x2 row_stats(const f32x2* __restrict__ st, int row) {
  const f32x4* p = (const f32x4*)(st + (size_t)row * NGRP);
  float ms[NGRP], m2 = 0.f, mean = 0.f;
#pragma unroll
  for (int i = 0; i < NGRP / 2; ++i) {
    f32x4 v = p[i];
    ms[2 * i] = v[0]; ms[2 * i + 1] = v[2];
    m2 += v[1] + v[3];
    mean += v[0] + v[2];
  }
  mean *= (1.f / NGRP);
  float d2 = 0.f;
#pragma unroll
  for (int i = 0; i < NGRP; ++i) { float d = ms[i] - mean; d2 += d * d; }
  const float var = (m2 + 32.f * d2) * (1.f / DM);
  const float mb = bfr(mean), vb = bfr(var);
  const float sd = bfr(sqrtf(bfr(vb + 1e-5f)));
  f32x2 r; r[0] = mb; r[1] = 1.f / sd;
  return r;
}

__global__ __launch_bounds__(256) void k_prep(const __bf16* __restrict__ x, const __bf16* __restrict__ wqkv,
                                              const __bf16* __restrict__ w1, const __bf16* __restrict__ w3,
                                              float* __restrict__ csum, f32x2* __restrict__ stats) {
  __shared__ float red[32][65];
  const int b = blockIdx.x, tid = threadIdx.x;
  if (b < 160) {
    const __bf16* W; int ld, col0, out0;
    if (b < 48) { W = wqkv; ld = 3 * DM; col0 = 64 * b; out0 = col0; }
    else if (b < 104) { W = w1; ld = FF; col0 = 64 * (b - 48); out0 = 3 * DM + col0; }
    else { W = w3; ld = FF; col0 = 64 * (b - 104); out0 = 3 * DM + FF + col0; }
    const int cc = tid & 7, rp = tid >> 3;
    float acc[8] = {0, 0, 0, 0, 0, 0, 0, 0};
#pragma unroll 4
    for (int r = rp; r < DM; r += 32) {
      bf16x8 v = *(const bf16x8*)(W + (size_t)r * ld + col0 + 8 * cc);
#pragma unroll
      for (int j = 0; j < 8; ++j) acc[j] += (float)v[j];
    }
#pragma unroll
    for (int j = 0; j < 8; ++j) red[rp][8 * cc + j] = acc[j];
    __syncthreads();
    if (tid < 64) {
      float s = 0.f;
#pragma unroll
      for (int i = 0; i < 32; ++i) s += red[i][tid];
      csum[out0 + tid] = s;
    }
  } else {
    const int id = (b - 160) * 256 + tid, row = id >> 5, grp = id & 31;
    const bf16x8* p = (const bf16x8*)(x + (size_t)row * DM + 32 * grp);
    float v[32]; float s = 0.f;
#pragma unroll
    for (int i = 0; i < 4; ++i) {
      bf16x8 t = p[i];
#pragma unroll
      for (int j = 0; j < 8; ++j) { v[8 * i + j] = (float)t[j]; s += v[8 * i + j]; }
    }
    const float mean = s * (1.f / 32.f);
    float m2 = 0.f;
#pragma unroll
    for (int i = 0; i < 32; ++i) { float d = v[i] - mean; m2 += d * d; }
    f32x2 r; r[0] = mean; r[1] = m2;
    stats[(size_t)row * NGRP + grp] = r;
  }
}

namespace p1 {
constexpr int R = 3;
constexpr int A_BYTES = 144 * 128, B_BYTES = 3 * 8192, SLOT = A_BYTES + B_BYTES;
constexpr int QS = 400;
constexpr int STAT_OFF = R * SLOT;
constexpr int LDS_BYTES = STAT_OFF + 144 * 8;
struct Frag { bf16x8 a[4]; bf16x8 ah; bf16x8 b[3]; };
}

__global__ __launch_bounds__(512, 2) void k_qkv_attn(const __bf16* __restrict__ x, const f32x2* __restrict__ stats,
                                                     const float* __restrict__ csum, const __bf16* __restrict__ wqkv,
                                                     __bf16* __restrict__ o) {
  using namespace p1;
  extern __shared__ __attribute__((aligned(16))) char smem_[];
  LDS3 char* sm = (LDS3 char*)smem_;
  const int tid = threadIdx.x, lane = tid & 63;
  const int w = __builtin_amdgcn_readfirstlane(tid >> 6);
  const int wm = w >> 2, wn = w & 3;
  const int b = blockIdx.x, xcd = b & 7, idx = b >> 3;
  const int mt = 8 * (xcd & 1) + (idx & 7), nt = 4 * (xcd >> 1) + (idx >> 3);
  const int m0 = mt * 128;
  TS(0, 0);

  uint32_t aoff[3], boff[3];
#pragma unroll
  for (int n = 0; n < 3; ++n) {
    const int j = w + 8 * n, row = 8 * j + (lane >> 3), c = (lane & 7) ^ (lane >> 3);
    int grow = m0 - 16 + row; grow = grow < 0 ? 0 : grow;
    aoff[n] = (uint32_t)(grow * DM + 8 * c) * 2u;
  }
#pragma unroll
  for (int n = 0; n < 3; ++n) {
    const int ii = 3 * w + n, panel = ii >> 3, blk = ii & 7;
    const int k = 8 * blk + (lane >> 3), f = (k & 2) | ((k >> 1) & 4), c = (lane & 7) ^ f;
    boff[n] = (uint32_t)(k * (3 * DM) + panel * DM + 64 * nt + 8 * c) * 2u;
  }
  auto load_stage = [&](int s, int slot) {
    LDS3 char* sb = sm + slot * SLOT;
    const char* ga = (const char*)x + s * 128;
#pragma unroll
    for (int n = 0; n < 3; ++n)
      if (n < 2 || w < 2) glds16(ga, aoff[n], sb + 1024 * (w + 8 * n));
    const char* gb = (const char*)wqkv + (size_t)s * (64 * 3 * DM * 2);
#pragma unroll
    for (int n = 0; n < 3; ++n) glds16(gb, boff[n], sb + A_BYTES + 1024 * (3 * w + n));
  };
  const int g = lane >> 4, q = (lane & 15) >> 2, p = lane & 3;
  const int laneA0 = 128 * (lane & 15) + 16 * (g ^ (lane & 7));
  const int laneA1 = laneA0 ^ 64;
  const int fh = ((q >> 1) & 1) + 2 * (g & 1);
  const int laneB = 128 * (8 * g + q) + 32 * (wn ^ fh) + 16 * (p >> 1) + 8 * (p & 1);
  auto read_frags = [&](int slot, int kk, Frag& F) {
    const LDS3 char* sa = sm + slot * SLOT + (kk ? laneA1 : laneA0);
#pragma unroll
    for (int i = 0; i < 4; ++i) F.a[i] = lds_b128(sa + 2048 * (1 + 4 * wm + i));
    F.ah = lds_b128(sa);
    const LDS3 char* sb = sm + slot * SLOT + A_BYTES + laneB + 4096 * kk;
#pragma unroll
    for (int pn = 0; pn < 3; ++pn) F.b[pn] = lds_tr2(sb + 8192 * pn, 512);
  };
  f32x4 acc[3][4], acch;
#pragma unroll
  for (int pn = 0; pn < 3; ++pn)
#pragma unroll
    for (int i = 0; i < 4; ++i) acc[pn][i] = f32x4{0, 0, 0, 0};
  acch = f32x4{0, 0, 0, 0};
  auto mma = [&](Frag& F) {
#pragma unroll
    for (int pn = 0; pn < 3; ++pn)
#pragma unroll
      for (int i = 0; i < 4; ++i)
        acc[pn][i] = __builtin_amdgcn_mfma_f32_16x16x32_bf16(F.b[pn], F.a[i], acc[pn][i], 0, 0, 0);
    const bf16x8 bh = wm ? F.b[2] : F.b[1];
    acch = __builtin_amdgcn_mfma_f32_16x16x32_bf16(bh, F.ah, acch, 0, 0, 0);
  };

  load_stage(0, 0);
  LDS3 f32x2* rowst = (LDS3 f32x2*)(sm + STAT_OFF);
  if (tid < 144) {
    int grow = m0 - 16 + tid; grow = grow < 0 ? 0 : grow;
    rowst[tid] = row_stats(stats, grow);
  }
  wait_vm<0>();
#pragma unroll
  for (int s = 1; s < R; ++s) load_stage(s, s);
  wait_lgkm0();
  bar();
  TS(0, 1);
  Frag X, Y;
  mainloop<R, 5>(DM / 64, X, Y, load_stage, read_frags, mma);
  TS(0, 2);
  wait_lgkm0();
  __syncthreads();

  {
    const int nl = 4 * g;
#pragma unroll
    for (int pn = 0; pn < 3; ++pn) {
      const f32x4 cs = *(const f32x4*)(csum + pn * DM + 64 * nt + 16 * wn + nl);
#pragma unroll
      for (int i = 0; i < 4; ++i) {
        const int row = 16 + 64 * wm + 16 * i + (lane & 15);
        const f32x2 rs = rowst[row];
        bf16x4 ov;
#pragma unroll
        for (int r = 0; r < 4; ++r) ov[r] = (__bf16)((acc[pn][i][r] - rs[0] * cs[r]) * rs[1]);
        *(LDS3 bf16x4*)(sm + row * QS + (64 * pn + 16 * wn + nl) * 2) = ov;
      }
    }
    {
      const int pn = wm ? 2 : 1;
      const f32x4 cs = *(const f32x4*)(csum + pn * DM + 64 * nt + 16 * wn + nl);
      const int row = lane & 15;
      const f32x2 rs = rowst[row];
      bf16x4 ov;
#pragma unroll
      for (int r = 0; r < 4; ++r) ov[r] = (__bf16)((acch[r] - rs[0] * cs[r]) * rs[1]);
      *(LDS3 bf16x4*)(sm + row * QS + (64 * pn + 16 * wn + nl) * 2) = ov;
    }
  }
  __syncthreads();

  {
    const int row = tid >> 2, head = (tid >> 1) & 1, half = tid & 1;
    const int s = m0 + row;
    const int cb = (32 * head + 16 * half) * 2;
    const LDS3 char* qp = sm + (16 + row) * QS + cb;
    const bf16x8 q0 = lds_b128(qp), q1 = lds_b128(qp + 16);
    float qf[16];
#pragma unroll
    for (int j = 0; j < 8; ++j) { qf[j] = (float)q0[j]; qf[8 + j] = (float)q1[j]; }
    float lg[8];
#pragma unroll
    for (int wi = 0; wi < 8; ++wi) {
      const LDS3 char* kp = sm + (16 + row - 2 * wi) * QS + 128 + cb;
      const bf16x8 k0 = lds_b128(kp), k1 = lds_b128(kp + 16);
      float d = 0.f;
#pragma unroll
      for (int j = 0; j < 8; ++j) { d += qf[j] * (float)k0[j]; d += qf[8 + j] * (float)k1[j]; }
      d += __shfl_xor(d, 1);
      const float l = bfr(bfr(d) * 0.17677669529663687f);
      lg[wi] = (s - 2 * wi >= 0) ? l : -1e9f;
    }
    float mx = lg[0];
#pragma unroll
    for (int wi = 1; wi < 8; ++wi) mx = fmaxf(mx, lg[wi]);
    float e[8], sum = 0.f;
#pragma unroll
    for (int wi = 0; wi < 8; ++wi) { e[wi] = __expf(lg[wi] - mx); sum += e[wi]; }
    const float inv = 1.f / sum;
    float ov[16];
#pragma unroll
    for (int j = 0; j < 16; ++j) ov[j] = 0.f;
#pragma unroll
    for (int wi = 0; wi < 8; ++wi) {
      const float pw = bfr(e[wi] * inv);
      const LDS3 char* vp = sm + (16 + row - 2 * wi) * QS + 256 + cb;
      const bf16x8 v0 = lds_b128(vp), v1 = lds_b128(vp + 16);
#pragma unroll
      for (int j = 0; j < 8; ++j) { ov[j] += pw * (float)v0[j]; ov[8 + j] += pw * (float)v1[j]; }
    }
    bf16x8 o0, o1;
#pragma unroll
    for (int j = 0; j < 8; ++j) { o0[j] = (__bf16)ov[j]; o1[j] = (__bf16)ov[8 + j]; }
    bf16x8* op = (bf16x8*)(o + (size_t)s * DM + 64 * nt + 32 * head + 16 * half);
    op[0] = o0; op[1] = o1;
  }
  TS(0, 3);
}

namespace p2 {
constexpr int R = 4;
constexpr int A_BYTES = 128 * 128, B_BYTES = 8192, SLOT = A_BYTES + B_BYTES;
constexpr int LDS_BYTES = R * SLOT;
struct Frag { bf16x8 a[2]; bf16x8 b[2]; };
}

__global__ __launch_bounds__(512, 2) void k_proj_res(const __bf16* __restrict__ A, const __bf16* __restrict__ W, int K,
                                                     const __bf16* __restrict__ xin, __bf16* __restrict__ xout,
                                                     f32x2* __restrict__ stats) {
  using namespace p2;
  extern __shared__ __attribute__((aligned(16))) char smem_[];
  LDS3 char* sm = (LDS3 char*)smem_;
  const int tid = threadIdx.x, lane = tid & 63;
  const int w = __builtin_amdgcn_readfirstlane(tid >> 6);
  const int wm = w >> 1, wn = w & 1;
  const int b = blockIdx.x, xcd = b & 7, idx = b >> 3;
  const int mt = 8 * (xcd & 1) + (idx & 7), nt = 4 * (xcd >> 1) + (idx >> 3);
  const int m0 = mt * 128, n0 = nt * 64;
  TS(1, 0);

  uint32_t aoff[2], boff;
#pragma unroll
  for (int n = 0; n < 2; ++n) {
    const int j = 2 * w + n, row = 8 * j + (lane >> 3), c = (lane & 7) ^ (lane >> 3);
    aoff[n] = (uint32_t)((m0 + row) * K + 8 * c) * 2u;
  }
  {
    const int k = 8 * w + (lane >> 3), f = (k & 2) | ((k >> 1) & 4), c = (lane & 7) ^ f;
    boff = (uint32_t)(k * DM + n0 + 8 * c) * 2u;
  }
  auto load_stage = [&](int s, int slot) {
    LDS3 char* sb = sm + slot * SLOT;
    const char* ga = (const char*)A + s * 128;
#pragma unroll
    for (int n = 0; n < 2; ++n) glds16(ga, aoff[n], sb + 1024 * (2 * w + n));
    const char* gb = (const char*)W + (size_t)s * (64 * DM * 2);
    glds16(gb, boff, sb + A_BYTES + 1024 * w);
  };
  const int g = lane >> 4, q = (lane & 15) >> 2, p = lane & 3;
  const int laneA0 = 128 * (lane & 15) + 16 * (g ^ (lane & 7));
  const int laneA1 = laneA0 ^ 64;
  const int fh = ((q >> 1) & 1) + 2 * (g & 1);
  int laneB[2];
#pragma unroll
  for (int jn = 0; jn < 2; ++jn) laneB[jn] = 128 * (8 * g + q) + 32 * ((2 * wn + jn) ^ fh) + 16 * (p >> 1) + 8 * (p & 1);
  auto read_frags = [&](int slot, int kk, Frag& F) {
    const LDS3 char* sa = sm + slot * SLOT + (kk ? laneA1 : laneA0);
#pragma unroll
    for (int i = 0; i < 2; ++i) F.a[i] = lds_b128(sa + 2048 * (2 * wm + i));
    const LDS3 char* sb = sm + slot * SLOT + A_BYTES + 4096 * kk;
#pragma unroll
    for (int jn = 0; jn < 2; ++jn) F.b[jn] = lds_tr2(sb + laneB[jn], 512);
  };
  f32x4 acc[2][2];
#pragma unroll
  for (int jn = 0; jn < 2; ++jn)
#pragma unroll
    for (int i = 0; i < 2; ++i) acc[jn][i] = f32x4{0, 0, 0, 0};
  auto mma = [&](Frag& F) {
#pragma unroll
    for (int jn = 0; jn < 2; ++jn)
#pragma unroll
      for (int i = 0; i < 2; ++i)
        acc[jn][i] = __builtin_amdgcn_mfma_f32_16x16x32_bf16(F.b[jn], F.a[i], acc[jn][i], 0, 0, 0);
  };
  const int nstages = K / 64;
#pragma unroll
  for (int s = 0; s < R; ++s) load_stage(s, s);
  wait_vm<(R - 1) * 3>();
  bar();
  TS(1, 1);
  Frag X, Y;
  mainloop<R, 3>(nstages, X, Y, load_stage, read_frags, mma);
  TS(1, 2);

  bf16x4 xo[2][2];
#pragma unroll
  for (int i = 0; i < 2; ++i)
#pragma unroll
    for (int jn = 0; jn < 2; ++jn)
      xo[i][jn] = *(const bf16x4*)(xin + (size_t)(m0 + 32 * wm + 16 * i + (lane & 15)) * DM + n0 + 32 * wn + 16 * jn + 4 * g);
#pragma unroll
  for (int i = 0; i < 2; ++i) {
    const int row = m0 + 32 * wm + 16 * i + (lane & 15);
    float v[8];
#pragma unroll
    for (int jn = 0; jn < 2; ++jn) {
      bf16x4 xn;
#pragma unroll
      for (int r = 0; r < 4; ++r) {
        xn[r] = (__bf16)((float)xo[i][jn][r] + bfr(acc[jn][i][r]));
        v[4 * jn + r] = (float)xn[r];
      }
      *(bf16x4*)(xout + (size_t)row * DM + n0 + 32 * wn + 16 * jn + 4 * g) = xn;
    }
    float s = 0.f;
#pragma unroll
    for (int j = 0; j < 8; ++j) s += v[j];
    s += __shfl_xor(s, 16); s += __shfl_xor(s, 32);
    const float mean = s * (1.f / 32.f);
    float m2 = 0.f;
#pragma unroll
    for (int j = 0; j < 8; ++j) { const float d = v[j] - mean; m2 += d * d; }
    m2 += __shfl_xor(m2, 16); m2 += __shfl_xor(m2, 32);
    if (g == 0) { f32x2 r; r[0] = mean; r[1] = m2; stats[(size_t)row * NGRP + 2 * nt + wn] = r; }
  }
  TS(1, 3);
}

namespace p3 {
constexpr int R = 2;
constexpr int A_BYTES = 128 * 128, G_BYTES = 64 * 448, U_OFF = G_BYTES + 16;
constexpr int SLOT = A_BYTES + U_OFF + G_BYTES;
constexpr int STAT_OFF = R * SLOT;
constexpr int LDS_BYTES = STAT_OFF + 128 * 8 + 448 * 4;
}

__global__ __launch_bounds__(512, 2) void k_ffn_up(const __bf16* __restrict__ x, const f32x2* __restrict__ stats,
                                                   const float* __restrict__ csum13, const __bf16* __restrict__ w1,
                                                   const __bf16* __restrict__ w3, __bf16* __restrict__ h) {
  using namespace p3;
  extern __shared__ __attribute__((aligned(16))) char smem_[];
  LDS3 char* sm = (LDS3 char*)smem_;
  const int tid = threadIdx.x, lane = tid & 63;
  const int w = __builtin_amdgcn_readfirstlane(tid >> 6);
  const int wm = w >> 2, wn = w & 3;
  const int b = blockIdx.x, xcd = b & 7, idx = b >> 3;
  const int mt = 8 * (xcd & 1) + (idx & 7), nt = 4 * (xcd >> 1) + (idx >> 3);
  const int m0 = mt * 128, n0 = nt * 224;
  TS(2, 0);

  uint32_t aoff[2], boff[7];
#pragma unroll
  for (int n = 0; n < 2; ++n) {
    const int j = 2 * w + n, row = 8 * j + (lane >> 3), c = (lane & 7) ^ (lane >> 3);
    aoff[n] = (uint32_t)((m0 + row) * DM + 8 * c) * 2u;
  }
  const __bf16* wsrc = (w >> 2) ? w3 : w1;
#pragma unroll
  for (int n = 0; n < 7; ++n) {
    const int id = 64 * (7 * (w & 3) + n) + lane, k = id / 28, cp = id % 28, c = cp ^ (2 * ((k >> 3) & 1));
    boff[n] = (uint32_t)(k * FF + n0 + 8 * c) * 2u;
  }
  auto load_stage = [&](int s, int slot) {
    LDS3 char* sb = sm + slot * SLOT;
    const char* ga = (const char*)x + s * 128;
#pragma unroll
    for (int n = 0; n < 2; ++n) glds16(ga, aoff[n], sb + 1024 * (2 * w + n));
    const char* gb = (const char*)wsrc + (size_t)s * (64 * FF * 2);
    LDS3 char* bb = sb + A_BYTES + (w >> 2) * U_OFF;
#pragma unroll
    for (int n = 0; n < 7; ++n) glds16(gb, boff[n], bb + 1024 * (7 * (w & 3) + n));
  };
  const int g = lane >> 4, q = (lane & 15) >> 2, p = lane & 3;
  const int laneA0 = 128 * (lane & 15) + 16 * (g ^ (lane & 7));
  const int laneA1 = laneA0 ^ 64;
  int laneB[7];
#pragma unroll
  for (int jj = 0; jj < 7; ++jj)
    laneB[jj] = (p < 2 ? 0 : U_OFF) + 448 * (8 * g + q) + 16 * ((7 * wn + jj) ^ (2 * (g & 1))) + 8 * (p & 1);
  auto rdA = [&](int slot, int kk, bf16x8 (&a)[4]) {
    const LDS3 char* sa = sm + slot * SLOT + (kk ? laneA1 : laneA0);
#pragma unroll
    for (int i = 0; i < 4; ++i) a[i] = lds_b128(sa + 2048 * (4 * wm + i));
  };
  auto rdB = [&](int slot, int kk, int jj) -> bf16x8 {
    return lds_tr2(sm + slot * SLOT + A_BYTES + 14336 * kk + laneB[jj], 1792);
  };
  f32x4 acc[7][4];
#pragma unroll
  for (int jj = 0; jj < 7; ++jj)
#pragma unroll
    for (int i = 0; i < 4; ++i) acc[jj][i] = f32x4{0, 0, 0, 0};
  load_stage(0, 0);
  LDS3 f32x2* rowst = (LDS3 f32x2*)(sm + STAT_OFF);
  LDS3 float* csl = (LDS3 float*)(sm + STAT_OFF + 1024);
  if (tid < 128) rowst[tid] = row_stats(stats, m0 + tid);
  else if (tid < 128 + 112) {
    const int t = tid - 128, m = t / 56, c4 = t % 56;
    *(LDS3 f32x4*)(csl + 224 * m + 4 * c4) = *(const f32x4*)(csum13 + m * FF + n0 + 4 * c4);
  }
  wait_vm<0>();
  load_stage(1, 1);
  wait_lgkm0();
  bar();
  TS(2, 1);
  {
    constexpr int NS = DM / 64;
    bf16x8 A0[4], A1[4];
    rdA(0, 0, A0);
    int slot = 0;
    for (int s = 0; s < NS; ++s) {
      const int nslot = slot ^ 1;
      rdA(slot, 1, A1);
      {
        bf16x8 b0 = rdB(slot, 0, 0), b1 = rdB(slot, 0, 1);
#pragma unroll
        for (int jj = 0; jj < 7; ++jj) {
          bf16x8 bn = b1;
          if (jj + 2 < 7) bn = rdB(slot, 0, jj + 2);
#pragma unroll
          for (int i = 0; i < 4; ++i)
            acc[jj][i] = __builtin_amdgcn_mfma_f32_16x16x32_bf16(b0, A0[i], acc[jj][i], 0, 0, 0);
          b0 = b1; b1 = bn;
          __builtin_amdgcn_sched_barrier(0);
        }
      }
      {
        bf16x8 b0 = rdB(slot, 1, 0), b1 = rdB(slot, 1, 1);
#pragma unroll
        for (int jj = 0; jj < 7; ++jj) {
          bf16x8 bn = b1;
          if (jj + 2 < 7) bn = rdB(slot, 1, jj + 2);
          if (jj == 5 && s + 1 < NS) {
            wait_lgkm0();
            wait_vm<0>();
            bar();
            if (s + 2 < NS) load_stage(s + 2, slot);
            rdA(nslot, 0, A0);
          }
#pragma unroll
          for (int i = 0; i < 4; ++i)
            acc[jj][i] = __builtin_amdgcn_mfma_f32_16x16x32_bf16(b0, A1[i], acc[jj][i], 0, 0, 0);
          b0 = b1; b1 = bn;
          __builtin_amdgcn_sched_barrier(0);
        }
      }
      slot = nslot;
    }
  }

  TS(2, 2);
  const int hi = lane >> 5, cq = 4 * (g & 1);
#pragma unroll
  for (int jj = 0; jj < 7; jj += 2) {
    const bool pair = (jj + 1 < 7);
    const int jb = pair ? jj + 1 : jj;
    const f32x4 csa = *(const LDS3 f32x4*)(csl + 224 * hi + 8 * (7 * wn + jj) + cq);
    const f32x4 csb = *(const LDS3 f32x4*)(csl + 224 * hi + 8 * (7 * wn + jb) + cq);
#pragma unroll
    for (int i = 0; i < 4; ++i) {
      const int lrow = 64 * wm + 16 * i + (lane & 15);
      const f32x2 rs = rowst[lrow];
      __bf16* hrow = h + (size_t)(m0 + lrow) * FF + n0 + 56 * wn + cq;
      bf16x4 hv;
#pragma unroll
      for (int r = 0; r < 4; ++r) {
        const float va = (acc[jj][i][r] - rs[0] * csa[r]) * rs[1];
        const float vb = (acc[jb][i][r] - rs[0] * csb[r]) * rs[1];
        unsigned ua = __builtin_bit_cast(unsigned, va), ub = __builtin_bit_cast(unsigned, vb);
        asm volatile("s_nop 1\n\tv_permlane32_swap_b32 %0, %1" : "+v"(ua), "+v"(ub));
        const float gt = bfr(__builtin_bit_cast(float, ua));
        const float up = bfr(__builtin_bit_cast(float, ub));
        const float sg = bfr(gt / (1.f + __expf(-gt)));
        hv[r] = (__bf16)(sg * up);
      }
      if (pair || hi == 0) *(bf16x4*)(hrow + 8 * (jj + hi)) = hv;
    }
  }
  TS(2, 3);
}

}

extern "C" void kernel_launch(void* const* d_in, const int* in_sizes, int n_in, void* d_out, int out_size, void* d_ws,
                              size_t ws_size, hipStream_t stream) {
  const __bf16* x0 = (const __bf16*)d_in[0];
  const __bf16* wqkv = (const __bf16*)d_in[1];
  const __bf16* wo = (const __bf16*)d_in[2];
  const __bf16* w1 = (const __bf16*)d_in[4];
  const __bf16* w3 = (const __bf16*)d_in[5];
  const __bf16* w2 = (const __bf16*)d_in[6];
  __bf16* out = (__bf16*)d_out;

  char* ws = (char*)d_ws;
  __bf16* o = (__bf16*)(ws);
  __bf16* xa = (__bf16*)(ws + (4 << 20));
  __bf16* xb = (__bf16*)(ws + (8 << 20));
  __bf16* h = (__bf16*)(ws + (12 << 20));
  f32x2* stats = (f32x2*)(ws + (28 << 20));
  float* csum = (float*)(ws + (29 << 20));

  hipFuncSetAttribute((const void*)k_qkv_attn, hipFuncAttributeMaxDynamicSharedMemorySize, p1::LDS_BYTES);
  hipFuncSetAttribute((const void*)k_proj_res, hipFuncAttributeMaxDynamicSharedMemorySize, p2::LDS_BYTES);
  hipFuncSetAttribute((const void*)k_ffn_up, hipFuncAttributeMaxDynamicSharedMemorySize, p3::LDS_BYTES);

  k_prep<<<416, 256, 0, stream>>>(x0, wqkv, w1, w3, csum, stats);
  const __bf16* xcur = x0;
  for (int layer = 0; layer < 2; ++layer) {
    __bf16* x1 = xa;
    __bf16* x2 = layer ? out : xb;
    k_qkv_attn<<<256, 512, p1::LDS_BYTES, stream>>>(xcur, stats, csum, wqkv, o);
    k_proj_res<<<256, 512, p2::LDS_BYTES, stream>>>(o, wo, DM, xcur, x1, stats);
    k_ffn_up<<<256, 512, p3::LDS_BYTES, stream>>>(x1, stats, csum + 3 * DM, w1, w3, h);
    k_proj_res<<<256, 512, p2::LDS_BYTES, stream>>>(h, w2, FF, x1, x2, stats);
    xcur = x2;
  }
}
```
